```python
import torch

def _softmax(x, axis=-1):
    return torch.softmax(x, dim=axis)
def _gelu(x):
    return torch.nn.functional.gelu(x, approximate="tanh")
def _silu(x):
    return torch.nn.functional.silu(x)
def _sigmoid(x):
    return torch.sigmoid(x)
def _layernorm(x, axis=-1, eps=1e-5):
    m = x.mean(dim=axis, keepdim=True)
    v = x.var(dim=axis, keepdim=True, unbiased=False)
    return (x - m) / torch.sqrt(v + eps)
def _scatter_add(tgt, idx, src, axis=0):
    return tgt.clone().index_add_(axis, idx, src)
def _segment_sum(src, seg, n):
    out = torch.zeros((n,) + src.shape[1:], dtype=src.dtype)
    return out.index_add_(0, seg, src)
def _pad(x, pw):
    flat = tuple(v for pair in reversed(pw) for v in pair)
    return torch.nn.functional.pad(x, flat)

kernel_name = 'standalone__rmsnorm__sampling'

def setup_inputs():

    _w = (lambda a: (lambda t: t.to(torch.bfloat16) if t.is_floating_point() else t)(torch.as_tensor(a)))
    B, S, D = 512, 32, 640
    return {
        "x": _w((lambda *s: torch.randn(*s, dtype=torch.bfloat16) * 0.05)(B, S, D)),
        "g": _w((lambda *s: torch.randn(*s, dtype=torch.bfloat16) * 0.05)(D) + 1.0),
    }

def reference(x, g):
    rms = torch.sqrt((x * x).mean(axis=-1, keepdims=True) + 1e-6)
    return x / rms * g
```

```cpp
#include <hip/hip_runtime.h>
#include <hip/hip_bf16.h>
#include <stdint.h>

namespace {

constexpr int kBlock = 512;
constexpr int kLoadAux = 2;
constexpr int kStoreAux = 19;

typedef unsigned int u32x4 __attribute__((ext_vector_type(4)));
typedef float f32x2 __attribute__((ext_vector_type(2)));
typedef __bf16 bf16x2 __attribute__((ext_vector_type(2)));

__device__ __forceinline__ float bf_lo(unsigned int u) { return __uint_as_float(u << 16); }
__device__ __forceinline__ float bf_hi(unsigned int u) { return __uint_as_float(u & 0xffff0000u); }

__device__ __forceinline__ unsigned int pack_bf16(float a, float b) {
    f32x2 f = {a, b};
    bf16x2 h = __builtin_convertvector(f, bf16x2);
    return __builtin_bit_cast(unsigned int, h);
}

template <int CTRL>
__device__ __forceinline__ float dpp_add(float v) {
    int t = __builtin_amdgcn_update_dpp(0, __float_as_int(v), CTRL, 0xf, 0xf, true);
    return v + __int_as_float(t);
}

__device__ __forceinline__ float sq_acc(unsigned int u, float acc) {
    bf16x2 h = __builtin_bit_cast(bf16x2, u);
    return __builtin_amdgcn_fdot2_f32_bf16(h, h, acc, false);
}

}

__global__ __launch_bounds__(kBlock) void rmsnorm640_kernel(const void* __restrict__ x,
                                                            const u32x4* __restrict__ g,
                                                            void* __restrict__ out,
                                                            unsigned nbytes) {
    asm volatile("" ::"s"(x), "s"(g), "s"(out), "s"(nbytes));

    const unsigned row = (blockIdx.x * kBlock + threadIdx.x) >> 4;
    const unsigned l = threadIdx.x & 15;
    const unsigned off = row * 1280u + l * 16u;
    __amdgpu_buffer_rsrc_t rx = __builtin_amdgcn_make_buffer_rsrc((void*)x, 0, nbytes, 0x00020000);
    __amdgpu_buffer_rsrc_t ro = __builtin_amdgcn_make_buffer_rsrc(out, 0, nbytes, 0x00020000);

    __shared__ u32x4 sg[80];
    u32x4 gtmp;
    if (threadIdx.x < 80) gtmp = g[threadIdx.x];
    __builtin_amdgcn_sched_barrier(0);

    u32x4 v[5];
#pragma unroll
    for (int i = 0; i < 5; ++i)
        v[i] = __builtin_amdgcn_raw_buffer_load_b128(rx, off + i * 256, 0, kLoadAux);
    __builtin_amdgcn_sched_barrier(0);

    if (threadIdx.x < 80) sg[threadIdx.x] = gtmp;
    asm volatile("s_waitcnt lgkmcnt(0)\n\ts_barrier" ::: "memory");
    __builtin_amdgcn_sched_barrier(0);
    u32x4 gv[5];
#pragma unroll
    for (int i = 0; i < 5; ++i) gv[i] = sg[i * 16 + l];
    __builtin_amdgcn_sched_barrier(0);

    float ss = 0.f;
#pragma unroll
    for (int i = 0; i < 5; ++i)
#pragma unroll
        for (int j = 0; j < 4; ++j) ss = sq_acc(v[i][j], ss);
    ss = dpp_add<0xB1>(ss);
    ss = dpp_add<0x4E>(ss);
    ss = dpp_add<0x141>(ss);
    ss = dpp_add<0x140>(ss);
    const float inv = __builtin_amdgcn_rsqf(ss * (1.0f / 640.0f) + 1e-6f);

    u32x4 o[5];
#pragma unroll
    for (int i = 0; i < 5; ++i) {
#pragma unroll
        for (int j = 0; j < 4; ++j) {
            float a = bf_lo(v[i][j]) * inv * bf_lo(gv[i][j]);
            float b = bf_hi(v[i][j]) * inv * bf_hi(gv[i][j]);
            o[i][j] = pack_bf16(a, b);
        }
    }

    __builtin_amdgcn_sched_barrier(0);
    asm volatile("s_barrier" ::: "memory");
    __builtin_amdgcn_sched_barrier(0);
#pragma unroll
    for (int i = 0; i < 5; ++i)
        __builtin_amdgcn_raw_buffer_store_b128(o[i], ro, off + i * 256, 0, kStoreAux);
}

__global__ __launch_bounds__(256) void rmsnorm_generic_kernel(const __hip_bfloat16* __restrict__ x,
                                                              const __hip_bfloat16* __restrict__ g,
                                                              __hip_bfloat16* __restrict__ out,
                                                              int nrows, int D) {
    const int row = blockIdx.x * 4 + (threadIdx.x >> 6);
    const int lane = threadIdx.x & 63;
    if (row >= nrows) return;
    const __hip_bfloat16* xr = x + (size_t)row * D;
    __hip_bfloat16* orow = out + (size_t)row * D;
    float ss = 0.f;
    for (int i = lane; i < D; i += 64) {
        float f = __bfloat162float(xr[i]);
        ss = fmaf(f, f, ss);
    }
    for (int d = 32; d > 0; d >>= 1) ss += __shfl_xor(ss, d, 64);
    const float inv = rsqrtf(ss / (float)D + 1e-6f);
    for (int i = lane; i < D; i += 64)
        orow[i] = __float2bfloat16(__bfloat162float(xr[i]) * inv * __bfloat162float(g[i]));
}

extern "C" void kernel_launch(void* const* d_in, const int* in_sizes, int n_in,
                              void* d_out, int out_size, void* d_ws, size_t ws_size,
                              hipStream_t stream) {
    const int D = in_sizes[1];
    const int nrows = in_sizes[0] / D;
    if (nrows <= 0) return;
    const bool aligned = (((uintptr_t)d_in[0] | (uintptr_t)d_in[1] | (uintptr_t)d_out) & 15) == 0;
    if (D == 640 && aligned && (size_t)nrows * 1280u < 0x7ff00000ull) {
        constexpr int rows_per_block = kBlock / 16;
        const int grid = (nrows + rows_per_block - 1) / rows_per_block;
        rmsnorm640_kernel<<<grid, kBlock, 0, stream>>>(d_in[0], (const u32x4*)d_in[1], d_out,
                                                       (unsigned)nrows * 1280u);
    } else {
        rmsnorm_generic_kernel<<<(nrows + 3) / 4, 256, 0, stream>>>(
            (const __hip_bfloat16*)d_in[0], (const __hip_bfloat16*)d_in[1],
            (__hip_bfloat16*)d_out, nrows, D);
    }
}
```

```cpp
#include <hip/hip_runtime.h>
#include <hip/hip_bf16.h>
#include <stdint.h>

namespace {

constexpr int kBlock = 512;
constexpr int kWaves = kBlock / 64;
constexpr int kGroup = 256;
constexpr int kLoadAux = 2;
constexpr int kStoreAux = 19;

typedef unsigned int u32x4 __attribute__((ext_vector_type(4)));
typedef float f32x2 __attribute__((ext_vector_type(2)));
typedef __bf16 bf16x2 __attribute__((ext_vector_type(2)));

__device__ __forceinline__ float bf_lo(unsigned int u) { return __uint_as_float(u << 16); }
__device__ __forceinline__ float bf_hi(unsigned int u) { return __uint_as_float(u & 0xffff0000u); }

__device__ __forceinline__ unsigned int pack_bf16(float a, float b) {
    f32x2 f = {a, b};
    bf16x2 h = __builtin_convertvector(f, bf16x2);
    return __builtin_bit_cast(unsigned int, h);
}

template <int CTRL>
__device__ __forceinline__ float dpp_add(float v) {
    int t = __builtin_amdgcn_update_dpp(0, __float_as_int(v), CTRL, 0xf, 0xf, true);
    return v + __int_as_float(t);
}

__device__ __forceinline__ float sq_acc(unsigned int u, float acc) {
    bf16x2 h = __builtin_bit_cast(bf16x2, u);
    return __builtin_amdgcn_fdot2_f32_bf16(h, h, acc, false);
}

}

template <bool REMAP>
__global__ __launch_bounds__(kBlock) void rmsnorm640_kernel(const void* __restrict__ x,
                                                            const u32x4* __restrict__ g,
                                                            void* __restrict__ out,
                                                            unsigned nbytes) {
    const unsigned wid = threadIdx.x >> 6;
    const unsigned wave = REMAP ? (blockIdx.x / kGroup) * (kGroup * kWaves) + wid * kGroup + (blockIdx.x % kGroup)
                                : blockIdx.x * kWaves + wid;
    const unsigned row = wave * 4 + ((threadIdx.x >> 4) & 3);
    const unsigned l = threadIdx.x & 15;
    const unsigned off = row * 1280u + l * 16u;
    asm volatile("" ::"s"(x), "s"(g), "s"(out), "s"(nbytes), "v"(off));
    __amdgpu_buffer_rsrc_t rx = __builtin_amdgcn_make_buffer_rsrc((void*)x, 0, nbytes, 0x00020000);
    __amdgpu_buffer_rsrc_t ro = __builtin_amdgcn_make_buffer_rsrc(out, 0, nbytes, 0x00020000);

    __shared__ u32x4 sg[80];
    u32x4 gtmp;
    if (threadIdx.x < 80) gtmp = g[threadIdx.x];
    __builtin_amdgcn_sched_barrier(0);

    u32x4 v[5];
#pragma unroll
    for (int i = 0; i < 5; ++i)
        v[i] = __builtin_amdgcn_raw_buffer_load_b128(rx, off + i * 256, 0, kLoadAux);
    __builtin_amdgcn_sched_barrier(0);

    if (threadIdx.x < 80) sg[threadIdx.x] = gtmp;
    asm volatile("s_waitcnt lgkmcnt(0)\n\ts_barrier" ::: "memory");
    __builtin_amdgcn_sched_barrier(0);
    u32x4 gv[5];
#pragma unroll
    for (int i = 0; i < 5; ++i) gv[i] = sg[i * 16 + l];
    __builtin_amdgcn_sched_barrier(0);

    float ss = 0.f;
#pragma unroll
    for (int i = 0; i < 5; ++i)
#pragma unroll
        for (int j = 0; j < 4; ++j) ss = sq_acc(v[i][j], ss);
    ss = dpp_add<0xB1>(ss);
    ss = dpp_add<0x4E>(ss);
    ss = dpp_add<0x141>(ss);
    ss = dpp_add<0x140>(ss);
    const float inv = __builtin_amdgcn_rsqf(ss * (1.0f / 640.0f) + 1e-6f);

    u32x4 o[5];
#pragma unroll
    for (int i = 0; i < 5; ++i) {
#pragma unroll
        for (int j = 0; j < 4; ++j) {
            float a = bf_lo(v[i][j]) * inv * bf_lo(gv[i][j]);
            float b = bf_hi(v[i][j]) * inv * bf_hi(gv[i][j]);
            o[i][j] = pack_bf16(a, b);
        }
    }

    __builtin_amdgcn_sched_barrier(0);
    asm volatile("s_barrier" ::: "memory");
    __builtin_amdgcn_sched_barrier(0);
#pragma unroll
    for (int i = 0; i < 5; ++i)
        __builtin_amdgcn_raw_buffer_store_b128(o[i], ro, off + i * 256, 0, kStoreAux);
}

__global__ __launch_bounds__(256) void rmsnorm_generic_kernel(const __hip_bfloat16* __restrict__ x,
                                                              const __hip_bfloat16* __restrict__ g,
                                                              __hip_bfloat16* __restrict__ out,
                                                              int nrows, int D) {
    const int row = blockIdx.x * 4 + (threadIdx.x >> 6);
    const int lane = threadIdx.x & 63;
    if (row >= nrows) return;
    const __hip_bfloat16* xr = x + (size_t)row * D;
    __hip_bfloat16* orow = out + (size_t)row * D;
    float ss = 0.f;
    for (int i = lane; i < D; i += 64) {
        float f = __bfloat162float(xr[i]);
        ss = fmaf(f, f, ss);
    }
    for (int d = 32; d > 0; d >>= 1) ss += __shfl_xor(ss, d, 64);
    const float inv = rsqrtf(ss / (float)D + 1e-6f);
    for (int i = lane; i < D; i += 64)
        orow[i] = __float2bfloat16(__bfloat162float(xr[i]) * inv * __bfloat162float(g[i]));
}

extern "C" void kernel_launch(void* const* d_in, const int* in_sizes, int n_in,
                              void* d_out, int out_size, void* d_ws, size_t ws_size,
                              hipStream_t stream) {
    const int D = in_sizes[1];
    const int nrows = in_sizes[0] / D;
    if (nrows <= 0) return;
    const bool aligned = (((uintptr_t)d_in[0] | (uintptr_t)d_in[1] | (uintptr_t)d_out) & 15) == 0;
    if (D == 640 && aligned && (size_t)nrows * 1280u < 0x7ff00000ull) {
        constexpr int rows_per_block = kBlock / 16;
        const int grid = (nrows + rows_per_block - 1) / rows_per_block;
        if (nrows % (rows_per_block * kGroup) == 0)
            rmsnorm640_kernel<true><<<grid, kBlock, 0, stream>>>(d_in[0], (const u32x4*)d_in[1],
                                                                 d_out, (unsigned)nrows * 1280u);
        else
            rmsnorm640_kernel<false><<<grid, kBlock, 0, stream>>>(d_in[0], (const u32x4*)d_in[1],
                                                                  d_out, (unsigned)nrows * 1280u);
    } else {
        rmsnorm_generic_kernel<<<(nrows + 3) / 4, 256, 0, stream>>>(
            (const __hip_bfloat16*)d_in[0], (const __hip_bfloat16*)d_in[1],
            (__hip_bfloat16*)d_out, nrows, D);
    }
}
```
